```python
import torch

def _softmax(x, axis=-1):
    return torch.softmax(x, dim=axis)
def _gelu(x):
    return torch.nn.functional.gelu(x, approximate="tanh")
def _silu(x):
    return torch.nn.functional.silu(x)
def _sigmoid(x):
    return torch.sigmoid(x)
def _layernorm(x, axis=-1, eps=1e-5):
    m = x.mean(dim=axis, keepdim=True)
    v = x.var(dim=axis, keepdim=True, unbiased=False)
    return (x - m) / torch.sqrt(v + eps)
def _scatter_add(tgt, idx, src, axis=0):
    return tgt.clone().index_add_(axis, idx, src)
def _segment_sum(src, seg, n):
    out = torch.zeros((n,) + src.shape[1:], dtype=src.dtype)
    return out.index_add_(0, seg, src)
def _pad(x, pw):
    flat = tuple(v for pair in reversed(pw) for v in pair)
    return torch.nn.functional.pad(x, flat)

kernel_name = 'standalone__weight_only_int_quip_2__prompt'

def setup_inputs():

    _w = (lambda a: (lambda t: t.to(torch.bfloat16) if t.is_floating_point() else t)(torch.as_tensor(a)))
    B, S, D, F = 1, 2048, 576, 4608
    nbits = max(2, min(4, int(4)))
    G = 64 if 64 > 0 else D
    D = (D // G) * G
    ng = D // G
    qmax = 1 << nbits
    qw = torch.randint(0, qmax, (D, F)).to(torch.int32)
    zp = torch.randint(0, qmax, (ng, F)).to(float)
    sc = torch.abs((lambda *s: torch.randn(*s, dtype=torch.bfloat16) * 0.05)(ng, F)).to(float) * 0.1 + 0.02
    return {
        "x": _w((lambda *s: torch.randn(*s, dtype=torch.bfloat16) * 0.05)(B, S, D)),
        "qw": _w(qw),
        "scales": _w(sc),
        "zeros": _w(zp),
    }

def reference(x, qw, scales, zeros):
    B, S, D = x.shape
    ng, F = scales.shape
    G = D // ng
    s = (lambda x, n, axis: torch.repeat_interleave(x, n, dim=axis))(scales, G, axis=0)
    z = (lambda x, n, axis: torch.repeat_interleave(x, n, dim=axis))(zeros, G, axis=0)
    w = ((lambda x, dt: x.to(dt))(qw, x.dtype) - z) * s
    return torch.einsum("bsd,df->bsf", x, w)
```

```cpp
#include <hip/hip_runtime.h>
#include <hip/hip_bf16.h>
#include <stdint.h>

typedef __attribute__((ext_vector_type(4))) unsigned u32x4;
typedef __attribute__((ext_vector_type(4))) int i32x4;
typedef __attribute__((ext_vector_type(2))) unsigned u32x2;
typedef __attribute__((ext_vector_type(4))) float f32x4;
typedef __attribute__((ext_vector_type(8))) short bf16x8;
typedef __attribute__((ext_vector_type(2))) __bf16 bf16x2_t;
typedef __attribute__((ext_vector_type(2))) float f32x2;

constexpr int M_ = 2048, K_ = 576, N_ = 4608, G_ = 64;
constexpr int BM = 256, BN = 144;

__device__ __forceinline__ unsigned pack_bf16(float a, float b) {
  f32x2 v = {a, b};
  bf16x2_t r = __builtin_convertvector(v, bf16x2_t);
  return __builtin_bit_cast(unsigned, r);
}
__device__ __forceinline__ float bf2f(unsigned short v) { return __builtin_bit_cast(float, (unsigned)v << 16); }

__global__ __launch_bounds__(512) void wq_gemm_v0(const unsigned short* __restrict__ x, const int* __restrict__ qw,
                                                  const unsigned short* __restrict__ sc, const unsigned short* __restrict__ zr,
                                                  unsigned short* __restrict__ out) {
  __shared__ __attribute__((aligned(16))) unsigned char sB[144 * 144];
  const int b = blockIdx.x;
  const int xcd = b & 7, idx = b >> 3;
  const int nt = xcd * 4 + (idx >> 3), mt = idx & 7;
  const int t = threadIdx.x, w = t >> 6, l = t & 63;
  const int r = l & 15, q = l >> 4;
  f32x4 acc[2][9] = {};
  const int n0 = nt * BN, m0 = mt * BM;
  for (int g = 0; g < 9; ++g) {
    if (t < 288) {
      int kg = t / 36, c4 = t % 36;
      int n = n0 + c4 * 4;
      i32x4 v[8];
#pragma unroll
      for (int j = 0; j < 8; ++j) v[j] = *(const i32x4*)(qw + (size_t)(g * 64 + kg * 8 + j) * N_ + n);
      u32x2 sv = *(const u32x2*)(sc + g * N_ + n);
      u32x2 zv = *(const u32x2*)(zr + g * N_ + n);
#pragma unroll
      for (int c = 0; c < 4; ++c) {
        float s = bf2f((unsigned short)(sv[c >> 1] >> ((c & 1) * 16)));
        float z = bf2f((unsigned short)(zv[c >> 1] >> ((c & 1) * 16)));
        float nzs = -z * s;
        u32x4 o;
#pragma unroll
        for (int j = 0; j < 4; ++j) {
          float a0 = __builtin_fmaf((float)v[2 * j][c], s, nzs);
          float a1 = __builtin_fmaf((float)v[2 * j + 1][c], s, nzs);
          o[j] = pack_bf16(a0, a1);
        }
        *(u32x4*)(sB + (c4 * 4 + c) * 144 + kg * 16) = o;
      }
    }
    bf16x8 a[2][2];
#pragma unroll
    for (int i = 0; i < 2; ++i)
#pragma unroll
      for (int s = 0; s < 2; ++s)
        a[i][s] = *(const bf16x8*)(x + (size_t)(m0 + w * 32 + i * 16 + r) * K_ + g * 64 + s * 32 + q * 8);
    __syncthreads();
#pragma unroll
    for (int s = 0; s < 2; ++s)
#pragma unroll
      for (int j = 0; j < 9; ++j) {
        bf16x8 bf = *(const bf16x8*)(sB + (j * 16 + r) * 144 + s * 64 + q * 16);
#pragma unroll
        for (int i = 0; i < 2; ++i)
          acc[i][j] = __builtin_amdgcn_mfma_f32_16x16x32_bf16(bf, a[i][s], acc[i][j], 0, 0, 0);
      }
    __syncthreads();
  }
#pragma unroll
  for (int i = 0; i < 2; ++i)
#pragma unroll
    for (int j = 0; j < 9; ++j) {
      u32x2 o;
      o[0] = pack_bf16(acc[i][j][0], acc[i][j][1]);
      o[1] = pack_bf16(acc[i][j][2], acc[i][j][3]);
      *(u32x2*)(out + (size_t)(m0 + w * 32 + i * 16 + r) * N_ + n0 + j * 16 + q * 4) = o;
    }
}

extern "C" void kernel_launch(void* const* d_in, const int* in_sizes, int n_in, void* d_out, int out_size, void* d_ws,
                              size_t ws_size, hipStream_t stream) {
  const unsigned short* x = (const unsigned short*)d_in[0];
  const int* qw = (const int*)d_in[1];
  const unsigned short* sc = (const unsigned short*)d_in[2];
  const unsigned short* zr = (const unsigned short*)d_in[3];
  wq_gemm_v0<<<256, 512, 0, stream>>>(x, qw, sc, zr, (unsigned short*)d_out);
}
```

```cpp
#include <hip/hip_runtime.h>
#include <hip/hip_bf16.h>
#include <stdint.h>

typedef __attribute__((ext_vector_type(4))) unsigned u32x4;
typedef __attribute__((ext_vector_type(4))) int i32x4;
typedef __attribute__((ext_vector_type(2))) unsigned u32x2;
typedef __attribute__((ext_vector_type(4))) float f32x4;
typedef __attribute__((ext_vector_type(8))) short bf16x8;
typedef __attribute__((ext_vector_type(2))) __bf16 bf16x2_t;
typedef __attribute__((ext_vector_type(2))) float f32x2;
#define AS3 __attribute__((address_space(3)))

#define ROT 0

constexpr int M_ = 2048, K_ = 576, N_ = 4608;
constexpr int QROW = N_ * 4;
constexpr int XROW = K_ * 2;
constexpr int NA = 3;
constexpr int PB = 3;
constexpr int LDS_A = 0;
constexpr int LDS_B = 8 * NA * 4096;
constexpr int LDS_S = LDS_B + 2 * 9 * 2048;
constexpr int LDS_TOTAL = LDS_S + 9 * 144 * 8;

__device__ __forceinline__ unsigned pack_bf16(float a, float b) {
  f32x2 v = {a, b};
  bf16x2_t r = __builtin_convertvector(v, bf16x2_t);
  return __builtin_bit_cast(unsigned, r);
}
__device__ __forceinline__ float fma1(float a, float b, float c) {
  float r;
  asm("v_fma_f32 %0, %1, %2, %3" : "=v"(r) : "v"(a), "v"(b), "v"(c));
  return r;
}
__device__ __forceinline__ float bflo(unsigned v) { return __builtin_bit_cast(float, v << 16); }
__device__ __forceinline__ float bfhi(unsigned v) { return __builtin_bit_cast(float, v & 0xffff0000u); }

struct Sched { int wS; int wB[9]; int wA[9]; };
constexpr Sched make_sched() {
  Sched s{};
  int endB[9] = {}, endA[9] = {}, endS = 0, n = 0;
  n += 2; endS = n;
  for (int i = 0; i < 3; ++i) { n += 6; endB[i] = n; n += 4; endA[i] = n; }
  s.wS = n - endS;
  s.wB[0] = n - endB[0];
  n += 6; endB[3] = n;
  for (int i = 0; i < 9; ++i) {
    if (i + 1 < 9) { s.wB[i + 1] = n - endB[i + 1]; if (i + 1 + PB < 9) { n += 6; endB[i + 1 + PB] = n; } }
    s.wA[i] = n - endA[i];
    if (i + NA < 9) { n += 4; endA[i + NA] = n; }
  }
  return s;
}
constexpr Sched SCH = make_sched();

struct BRegs { i32x4 q[4]; int t0, t1; };

__device__ __forceinline__ void issue_B(BRegs& r, const char* b4, const char* b1, unsigned voffQ, unsigned voff1) {
  const char* p1 = b4 + QROW; const char* p2 = b4 + 2 * QROW; const char* p3 = b4 + 3 * QROW; const char* b1b = b1 + QROW;
  asm volatile("global_load_dwordx4 %0, %1, %2" : "=v"(r.q[0]) : "v"(voffQ), "s"(b4) : "memory");
  asm volatile("global_load_dwordx4 %0, %1, %2" : "=v"(r.q[1]) : "v"(voffQ), "s"(p1) : "memory");
  asm volatile("global_load_dwordx4 %0, %1, %2" : "=v"(r.q[2]) : "v"(voffQ), "s"(p2) : "memory");
  asm volatile("global_load_dwordx4 %0, %1, %2" : "=v"(r.q[3]) : "v"(voffQ), "s"(p3) : "memory");
  asm volatile("global_load_dword %0, %1, %2" : "=v"(r.t0) : "v"(voff1), "s"(b1) : "memory");
  asm volatile("global_load_dword %0, %1, %2" : "=v"(r.t1) : "v"(voff1), "s"(b1b) : "memory");
}
template <int N>
__device__ __forceinline__ void wait_B(BRegs& r) {
  asm volatile("s_waitcnt vmcnt(%6)" : "+v"(r.q[0]), "+v"(r.q[1]), "+v"(r.q[2]), "+v"(r.q[3]), "+v"(r.t0), "+v"(r.t1) : "i"(N) : "memory");
}
__device__ __forceinline__ void issue_A(const char* xb, unsigned voffA, unsigned ldsdst) {
  const char* p1 = xb + 8 * XROW; const char* p2 = xb + 16 * XROW; const char* p3 = xb + 24 * XROW;
  unsigned keep;
  asm volatile(
      "s_mov_b32 %0, m0\n\t"
      "s_mov_b32 m0, %6\n\ts_nop 0\n\t"
      "global_load_lds_dwordx4 %1, %2\n\t"
      "s_add_u32 m0, m0, 0x400\n\ts_nop 0\n\t"
      "global_load_lds_dwordx4 %1, %3\n\t"
      "s_add_u32 m0, m0, 0x400\n\ts_nop 0\n\t"
      "global_load_lds_dwordx4 %1, %4\n\t"
      "s_add_u32 m0, m0, 0x400\n\ts_nop 0\n\t"
      "global_load_lds_dwordx4 %1, %5\n\t"
      "s_mov_b32 m0, %0"
      : "=&s"(keep) : "v"(voffA), "s"(xb), "s"(p1), "s"(p2), "s"(p3), "s"(ldsdst) : "memory", "scc");
}

__global__ __launch_bounds__(512, 2) void wq_gemm_v1(const unsigned short* __restrict__ x, const int* __restrict__ qw,
                                                     const unsigned short* __restrict__ sc, const unsigned short* __restrict__ zr,
                                                     unsigned short* __restrict__ out) {
  __shared__ __attribute__((aligned(16))) char smem[LDS_TOTAL];
  AS3 char* sm = (AS3 char*)smem;
  const unsigned lds0 = (unsigned)(size_t)sm;
  const int b = blockIdx.x;
  const int xcd = b & 7, idx = b >> 3;
  const int nt = xcd * 4 + (idx >> 3), mt = idx & 7;
  const int t = threadIdx.x, l = t & 63;
  const int w = __builtin_amdgcn_readfirstlane(t >> 6);
  const int r = l & 15, q = l >> 4;
  const int n0 = nt * 144, m0w = mt * 256 + w * 32;
  const int odd = nt & 1;
  const int rel4 = odd * 16 + (w >> 2) * 64;
  const int rel1 = odd ? 0 : 128;
  const int g0 = ROT ? (mt + nt) % 9 : 0;

  const int cq = (l & 7) | ((l >> 1) & 8), kq = ((l >> 3) & 1) | ((l >> 4) & 2);
  const unsigned voffQ = (unsigned)(kq * 4 * QROW + cq * 16);
  const unsigned voff1 = (unsigned)((q * 2) * QROW + r * 4);
  const unsigned voffA = (unsigned)((l >> 3) * XROW + (((l & 7) ^ (l >> 3)) * 16));
  const char* qb4 = (const char*)qw + (size_t)((w & 3) * 16) * QROW + (size_t)(n0 + rel4) * 4;
  const char* qb1 = (const char*)qw + (size_t)(w * 8) * QROW + (size_t)(n0 + rel1) * 4;
  const char* xb = (const char*)x + (size_t)m0w * XROW;
  const unsigned ldsA = lds0 + LDS_A + w * (NA * 4096);
  auto grp = [&](int i) { int g = g0 + i; return g >= 9 ? g - 9 : g; };

  u32x2 sv, zv;
  {
    int tt = t < 324 ? t : 323;
    unsigned so = (unsigned)((tt / 36) * N_ + n0 + (tt % 36) * 4) * 2;
    asm volatile("global_load_dwordx2 %0, %1, %2" : "=v"(sv) : "v"(so), "s"(sc) : "memory");
    asm volatile("global_load_dwordx2 %0, %1, %2" : "=v"(zv) : "v"(so), "s"(zr) : "memory");
  }
  BRegs br[PB];
#pragma unroll
  for (int i = 0; i < 3; ++i) {
    int g = grp(i);
    issue_B(br[i], qb4 + (size_t)g * 64 * QROW, qb1 + (size_t)g * 64 * QROW, voffQ, voff1);
    issue_A(xb + g * 128, voffA, ldsA + i * 4096);
  }
  asm volatile("s_waitcnt vmcnt(%2)" : "+v"(sv), "+v"(zv) : "i"(SCH.wS) : "memory");
  if (t < 324) {
    float s0 = bflo(sv[0]), s1 = bfhi(sv[0]), s2 = bflo(sv[1]), s3 = bfhi(sv[1]);
    float z0 = bflo(zv[0]), z1 = bfhi(zv[0]), z2 = bflo(zv[1]), z3 = bfhi(zv[1]);
    f32x4 a = {s0, -z0 * s0, s1, -z1 * s1}, c = {s2, -z2 * s2, s3, -z3 * s3};
    *(AS3 f32x4*)(sm + LDS_S + t * 32) = a;
    *(AS3 f32x4*)(sm + LDS_S + t * 32 + 16) = c;
  }
  asm volatile("s_waitcnt lgkmcnt(0)" ::: "memory");
  __builtin_amdgcn_s_barrier();
  asm volatile("" ::: "memory");

  const int kg = (w & 3) * 2 + (kq >> 1);
  const int wr4 = LDS_B + (w >> 2) * 4 * 2048 + cq * 128 + ((kg ^ (cq & 7)) * 16) + (kq & 1) * 8;
  const int wr1 = LDS_B + 8 * 2048 + r * 128 + ((w ^ (r & 7)) * 16) + q * 4;
  const int sc4 = LDS_S + (rel4 + cq * 4) * 8;
  const int sc1 = LDS_S + (rel1 + r) * 8;
  auto dequant = [&](BRegs& R, int g, int buf) {
    f32x4 sa = *(AS3 f32x4*)(sm + sc4 + g * 1152);
    f32x4 sb = *(AS3 f32x4*)(sm + sc4 + g * 1152 + 16);
    f32x2 s1 = *(AS3 f32x2*)(sm + sc1 + g * 1152);
    float ss[4] = {sa[0], sa[2], sb[0], sb[2]}, cc[4] = {sa[1], sa[3], sb[1], sb[3]};
#pragma unroll
    for (int c = 0; c < 4; ++c) {
      float v0 = fma1((float)R.q[0][c], ss[c], cc[c]);
      float v1 = fma1((float)R.q[1][c], ss[c], cc[c]);
      float v2 = fma1((float)R.q[2][c], ss[c], cc[c]);
      float v3 = fma1((float)R.q[3][c], ss[c], cc[c]);
      u32x2 o = {pack_bf16(v0, v1), pack_bf16(v2, v3)};
      *(AS3 u32x2*)(sm + wr4 + c * 2048 + buf * 18432) = o;
    }
    float u0 = fma1((float)R.t0, s1[0], s1[1]);
    float u1 = fma1((float)R.t1, s1[0], s1[1]);
    *(AS3 unsigned*)(sm + wr1 + buf * 18432) = pack_bf16(u0, u1);
  };

  wait_B<SCH.wB[0]>(br[0]);
  dequant(br[0], grp(0), 0);
  { int g = grp(3); issue_B(br[0], qb4 + (size_t)g * 64 * QROW, qb1 + (size_t)g * 64 * QROW, voffQ, voff1); }
  asm volatile("s_waitcnt lgkmcnt(0)" ::: "memory");
  __builtin_amdgcn_s_barrier();
  asm volatile("" ::: "memory");

  f32x4 acc[2][9] = {};
  const int fr0 = r * 128 + ((q ^ (r & 7)) * 16);
  const int fr1 = r * 128 + (((4 + q) ^ (r & 7)) * 16);
  const int rdA = LDS_A + w * (NA * 4096);

  auto interval = [&](auto IC) {
    constexpr int i = decltype(IC)::value;
    if constexpr (i + 1 < 9) {
      wait_B<SCH.wB[i + 1]>(br[(i + 1) % PB]);
      dequant(br[(i + 1) % PB], grp(i + 1), (i + 1) & 1);
      if constexpr (i + 1 + PB < 9) {
        int g = grp(i + 1 + PB);
        issue_B(br[(i + 1) % PB], qb4 + (size_t)g * 64 * QROW, qb1 + (size_t)g * 64 * QROW, voffQ, voff1);
      }
    }
    asm volatile("s_waitcnt vmcnt(%0)" ::"i"(SCH.wA[i]) : "memory");
    bf16x8 a[2][2];
#pragma unroll
    for (int mi = 0; mi < 2; ++mi) {
      a[mi][0] = *(AS3 bf16x8*)(sm + rdA + (i % NA) * 4096 + mi * 2048 + fr0);
      a[mi][1] = *(AS3 bf16x8*)(sm + rdA + (i % NA) * 4096 + mi * 2048 + fr1);
    }
    if constexpr (i + NA < 9) {
      asm volatile("s_waitcnt lgkmcnt(0)" : "+v"(a[0][0]), "+v"(a[0][1]), "+v"(a[1][0]), "+v"(a[1][1])::"memory");
      issue_A(xb + grp(i + NA) * 128, voffA, ldsA + (i % NA) * 4096);
    }
#pragma unroll
    for (int s = 0; s < 2; ++s)
#pragma unroll
      for (int j = 0; j < 9; ++j) {
        bf16x8 bf = *(AS3 bf16x8*)(sm + LDS_B + (i & 1) * 18432 + j * 2048 + (s ? fr1 : fr0));
#pragma unroll
        for (int mi = 0; mi < 2; ++mi)
          acc[mi][j] = __builtin_amdgcn_mfma_f32_16x16x32_bf16(a[mi][s], bf, acc[mi][j], 0, 0, 0);
      }
    if constexpr (i + 1 < 9) {
      asm volatile("s_waitcnt lgkmcnt(0)" ::: "memory");
      __builtin_amdgcn_s_barrier();
      asm volatile("" ::: "memory");
    }
  };
  interval(std::integral_constant<int, 0>{});
  interval(std::integral_constant<int, 1>{});
  interval(std::integral_constant<int, 2>{});
  interval(std::integral_constant<int, 3>{});
  interval(std::integral_constant<int, 4>{});
  interval(std::integral_constant<int, 5>{});
  interval(std::integral_constant<int, 6>{});
  interval(std::integral_constant<int, 7>{});
  interval(std::integral_constant<int, 8>{});

  unsigned short* ob = out + (size_t)(m0w + q * 4) * N_ + n0;
#pragma unroll
  for (int mi = 0; mi < 2; ++mi)
#pragma unroll
    for (int j = 0; j < 4; ++j) {
      unsigned short* orow = ob + (size_t)(mi * 16 + j) * N_;
#pragma unroll
      for (int G = 0; G < 2; ++G) {
        u32x2 o = {pack_bf16(acc[mi][G * 4 + 0][j], acc[mi][G * 4 + 1][j]), pack_bf16(acc[mi][G * 4 + 2][j], acc[mi][G * 4 + 3][j])};
        *(u32x2*)(orow + odd * 16 + G * 64 + r * 4) = o;
      }
      orow[rel1 + r] = (unsigned short)(pack_bf16(acc[mi][8][j], 0.f) & 0xffff);
    }
}

extern "C" void kernel_launch(void* const* d_in, const int* in_sizes, int n_in, void* d_out, int out_size, void* d_ws,
                              size_t ws_size, hipStream_t stream) {
  const unsigned short* x = (const unsigned short*)d_in[0];
  const int* qw = (const int*)d_in[1];
  const unsigned short* sc = (const unsigned short*)d_in[2];
  const unsigned short* zr = (const unsigned short*)d_in[3];
  wq_gemm_v1<<<256, 512, 0, stream>>>(x, qw, sc, zr, (unsigned short*)d_out);
}
```

```cpp
#include <hip/hip_runtime.h>
#include <hip/hip_bf16.h>
#include <stdint.h>
#include <type_traits>

typedef __attribute__((ext_vector_type(4))) unsigned u32x4;
typedef __attribute__((ext_vector_type(4))) int i32x4;
typedef __attribute__((ext_vector_type(2))) int i32x2;
typedef __attribute__((ext_vector_type(2))) unsigned u32x2;
typedef __attribute__((ext_vector_type(4))) float f32x4;
typedef __attribute__((ext_vector_type(8))) short bf16x8;
typedef __attribute__((ext_vector_type(2))) __bf16 bf16x2_t;
typedef __attribute__((ext_vector_type(2))) float f32x2;
#define AS3 __attribute__((address_space(3)))
#define ROT 0
#define BPOL ""
#define PPRIO 0
#define CPRIO 0
#define ACC(k, code) do{ code; }while(0)
#define MARK(k) do{}while(0)
#define STAMP() do{}while(0)
#define SB() __builtin_amdgcn_sched_barrier(0)

constexpr int M_ = 2048, K_ = 576, N_ = 4608;
constexpr int QROW = N_ * 4;
constexpr int XROW = K_ * 2;
constexpr int LDS_A = 0;
constexpr int LDS_B = 3 * 32768;
constexpr int LDS_S = LDS_B + 2 * 18432;
constexpr int LDS_TOTAL = LDS_S + 9 * 144 * 8;

__device__ __forceinline__ unsigned pack_bf16(float a, float b) {
  f32x2 v = {a, b};
  bf16x2_t r = __builtin_convertvector(v, bf16x2_t);
  return __builtin_bit_cast(unsigned, r);
}
__device__ __forceinline__ float fma1(float a, float b, float c) {
  float r;
  asm("v_fma_f32 %0, %1, %2, %3" : "=v"(r) : "v"(a), "v"(b), "v"(c));
  return r;
}
__device__ __forceinline__ float bflo(unsigned v) { return __builtin_bit_cast(float, v << 16); }
__device__ __forceinline__ float bfhi(unsigned v) { return __builtin_bit_cast(float, v & 0xffff0000u); }
__device__ __forceinline__ void bar() {
  asm volatile("" ::: "memory");
  __builtin_amdgcn_s_waitcnt(0xC07F);
  __builtin_amdgcn_s_barrier();
  asm volatile("" ::: "memory");
}
__device__ __forceinline__ void bar_v0() {
  asm volatile("" ::: "memory");
  __builtin_amdgcn_s_waitcnt(0x0070);
  __builtin_amdgcn_s_barrier();
  asm volatile("" ::: "memory");
}
__device__ __forceinline__ u32x4 deq8(int q0, int q1, int q2, int q3, int q4, int q5, int q6, int q7, float s, float c) {
  unsigned o0, o1, o2, o3; float t0, t1, t2, t3, t4, t5, t6, t7;
  asm("v_cvt_f32_i32 %4, %12\n\tv_cvt_f32_i32 %5, %13\n\tv_cvt_f32_i32 %6, %14\n\tv_cvt_f32_i32 %7, %15\n\t"
      "v_cvt_f32_i32 %8, %16\n\tv_cvt_f32_i32 %9, %17\n\tv_cvt_f32_i32 %10, %18\n\tv_cvt_f32_i32 %11, %19\n\t"
      "v_fma_f32 %4, %4, %20, %21\n\tv_fma_f32 %5, %5, %20, %21\n\tv_fma_f32 %6, %6, %20, %21\n\tv_fma_f32 %7, %7, %20, %21\n\t"
      "v_fma_f32 %8, %8, %20, %21\n\tv_fma_f32 %9, %9, %20, %21\n\tv_fma_f32 %10, %10, %20, %21\n\tv_fma_f32 %11, %11, %20, %21\n\t"
      "v_cvt_pk_bf16_f32 %0, %4, %5\n\tv_cvt_pk_bf16_f32 %1, %6, %7\n\tv_cvt_pk_bf16_f32 %2, %8, %9\n\tv_cvt_pk_bf16_f32 %3, %10, %11"
      : "=&v"(o0), "=&v"(o1), "=&v"(o2), "=&v"(o3), "=&v"(t0), "=&v"(t1), "=&v"(t2), "=&v"(t3), "=&v"(t4), "=&v"(t5), "=&v"(t6), "=&v"(t7)
      : "v"(q0), "v"(q1), "v"(q2), "v"(q3), "v"(q4), "v"(q5), "v"(q6), "v"(q7), "v"(s), "v"(c));
  u32x4 o = {o0, o1, o2, o3};
  return o;
}
__device__ __forceinline__ u32x2 deq4(int a0, int a1, int b0, int b1, float sa, float ca, float sb, float cb) {
  unsigned o0, o1; float t0, t1, t2, t3;
  asm("v_cvt_f32_i32 %2, %6\n\tv_cvt_f32_i32 %3, %7\n\tv_cvt_f32_i32 %4, %8\n\tv_cvt_f32_i32 %5, %9\n\t"
      "v_fma_f32 %2, %2, %10, %11\n\tv_fma_f32 %3, %3, %10, %11\n\tv_fma_f32 %4, %4, %12, %13\n\tv_fma_f32 %5, %5, %12, %13\n\t"
      "s_nop 0\n\tv_cvt_pk_bf16_f32 %0, %2, %3\n\tv_cvt_pk_bf16_f32 %1, %4, %5"
      : "=&v"(o0), "=&v"(o1), "=&v"(t0), "=&v"(t1), "=&v"(t2), "=&v"(t3)
      : "v"(a0), "v"(a1), "v"(b0), "v"(b1), "v"(sa), "v"(ca), "v"(sb), "v"(cb));
  u32x2 o = {o0, o1};
  return o;
}

struct Sched { int wB[9]; int wA[9]; };
constexpr Sched make_sched() {
  Sched s{};
  int endB[12] = {}, endA[12] = {}, n = 0;
  n += 10; endB[0] = n; n += 10; endB[1] = n;
  for (int i = -1; i < 8; ++i) {
    s.wB[i + 1] = n - endB[i + 1];
    if (i >= 0 && i + 2 < 9) { n += 8; endA[i + 2] = n; }
    if (i + 3 < 9) { n += 10; endB[i + 3] = n; }
    s.wA[i + 1] = (i >= 1) ? n - endA[i + 1] : 63;
  }
  return s;
}
constexpr Sched SCH = make_sched();

struct BRegs { i32x4 q[8]; i32x2 t[2]; };

template <int J0>
__device__ __forceinline__ void issue_B4(BRegs& r, const char* b4, unsigned voffQ) {
#pragma unroll
  for (int j = J0; j < J0 + 4; ++j) {
    const char* p = b4 + (size_t)j * QROW;
    asm volatile("global_load_dwordx4 %0, %1, %2" BPOL : "=v"(r.q[j]) : "v"(voffQ), "s"(p) : "memory");
  }
}
__device__ __forceinline__ void issue_B1(BRegs& r, const char* b1, unsigned voff1) {
  const char* b1b = b1 + QROW;
  asm volatile("global_load_dwordx2 %0, %1, %2" BPOL : "=v"(r.t[0]) : "v"(voff1), "s"(b1) : "memory");
  asm volatile("global_load_dwordx2 %0, %1, %2" BPOL : "=v"(r.t[1]) : "v"(voff1), "s"(b1b) : "memory");
}
template <int N>
__device__ __forceinline__ void wait_B(BRegs& r) {
  asm volatile("s_waitcnt vmcnt(%10)"
               : "+v"(r.q[0]), "+v"(r.q[1]), "+v"(r.q[2]), "+v"(r.q[3]), "+v"(r.q[4]), "+v"(r.q[5]), "+v"(r.q[6]), "+v"(r.q[7]),
                 "+v"(r.t[0]), "+v"(r.t[1])
               : "i"(N) : "memory");
}
__device__ __forceinline__ void issue_A4(const char* xb, unsigned voffA, unsigned ldsdst) {
  const char* p1 = xb + 8 * XROW; const char* p2 = xb + 16 * XROW; const char* p3 = xb + 24 * XROW;
  unsigned keep;
  asm volatile(
      "s_mov_b32 %0, m0\n\t"
      "s_mov_b32 m0, %6\n\ts_nop 0\n\t"
      "global_load_lds_dwordx4 %1, %2\n\t"
      "s_add_u32 m0, m0, 0x400\n\ts_nop 0\n\t"
      "global_load_lds_dwordx4 %1, %3\n\t"
      "s_add_u32 m0, m0, 0x400\n\ts_nop 0\n\t"
      "global_load_lds_dwordx4 %1, %4\n\t"
      "s_add_u32 m0, m0, 0x400\n\ts_nop 0\n\t"
      "global_load_lds_dwordx4 %1, %5\n\t"
      "s_mov_b32 m0, %0"
      : "=&s"(keep) : "v"(voffA), "s"(xb), "s"(p1), "s"(p2), "s"(p3), "s"(ldsdst) : "memory", "scc");
}

template <int I> using IC = std::integral_constant<int, I>;

__global__ __launch_bounds__(512, 2) void wq_gemm_v3(const unsigned short* __restrict__ x, const int* __restrict__ qw,
                                                     const unsigned short* __restrict__ sc, const unsigned short* __restrict__ zr,
                                                     unsigned short* __restrict__ out
) {
  __shared__ __attribute__((aligned(16))) char smem[LDS_TOTAL];
  AS3 char* sm = (AS3 char*)smem;
  const unsigned lds0 = (unsigned)(size_t)sm;
  const int b = blockIdx.x;
  const int xcd = b & 7, idx = b >> 3;
  const int nt = xcd * 4 + (idx >> 3), mt = idx & 7;
  const int t = threadIdx.x, l = t & 63;
  const int w = __builtin_amdgcn_readfirstlane(t >> 6);
  const int r = l & 15, q = l >> 4;
  const int n0 = nt * 144, m0 = mt * 256;
  const int odd = nt & 1;
  const int rel1 = odd ? 0 : 128;
  const int g0 = ROT ? (mt + nt) % 9 : 0;
  auto grp = [&](int i) { int g = g0 + i; return g >= 9 ? g - 9 : g; };
  const int p = w & 3;
  const unsigned voffA = (unsigned)((l >> 3) * XROW + (((l & 7) ^ (l >> 3)) * 16));
  const char* xb = (const char*)x + (size_t)(m0 + p * 64) * XROW;
  const unsigned ldsA = lds0 + LDS_A + p * 8192;

  if (w >= 4) {
    const int G = p >> 1, h = p & 1;
    const int rel4 = odd * 16 + G * 64;
    const int cq = r, kgl = q;
    const unsigned voffQ = (unsigned)(kgl * 8 * QROW + cq * 16);
    const int cp = l & 7, kp = l >> 3;
    const unsigned voff1 = (unsigned)(kp * 2 * QROW + cp * 8);
    const char* qb4 = (const char*)qw + (size_t)(h * 32) * QROW + (size_t)(n0 + rel4) * 4;
    const char* qb1 = (const char*)qw + (size_t)(p * 16) * QROW + (size_t)(n0 + rel1) * 4;
    BRegs br[3];
#pragma unroll
    for (int i = 0; i < 2; ++i) {
      size_t o = (size_t)grp(i) * 64 * QROW;
      issue_B4<0>(br[i], qb4 + o, voffQ); issue_B4<4>(br[i], qb4 + o, voffQ); issue_B1(br[i], qb1 + o, voff1);
    }
    if (PPRIO) __builtin_amdgcn_s_setprio(PPRIO);
    const int kg = h * 4 + kgl;
    const int wr4 = LDS_B + G * 4 * 2048 + cq * 128 + ((kg ^ (cq & 7)) * 16);
    const int kc1 = p * 2 + (kp >> 2);
    const int wr1a = LDS_B + 8 * 2048 + (cp * 2) * 128 + ((kc1 ^ ((cp * 2) & 7)) * 16) + (kp & 3) * 4;
    const int wr1b = LDS_B + 8 * 2048 + (cp * 2 + 1) * 128 + ((kc1 ^ ((cp * 2 + 1) & 7)) * 16) + (kp & 3) * 4;
    const int sc4 = LDS_S + (rel4 + cq * 4) * 8;
    const int sc1 = LDS_S + (rel1 + cp * 2) * 8;
    MARK(1);
    bar();
    MARK(2);
    auto interval = [&](auto ic) {
      constexpr int i = decltype(ic)::value;
      BRegs& R = br[(i + 1) % 3];
      BRegs& L = br[(i + 3) % 3];
      constexpr int buf = (i + 1) & 1;
      const int g = grp(i + 1);
      f32x4 sa = *(AS3 f32x4*)(sm + sc4 + g * 1152);
      f32x4 sb = *(AS3 f32x4*)(sm + sc4 + g * 1152 + 16);
      f32x4 s1 = *(AS3 f32x4*)(sm + sc1 + g * 1152);
      ACC(3, wait_B<SCH.wB[i + 1]>(R));
      constexpr bool doA = (i >= 0 && i + 2 < 9), doB = (i + 3 < 9);
      const char* sA = xb + grp(doA ? i + 2 : 0) * 128;
      const size_t oB = (size_t)grp(doB ? i + 3 : 0) * 64 * QROW;
      auto col = [&](auto cc_, float s, float c) {
        constexpr int c_ = decltype(cc_)::value;
        *(AS3 u32x4*)(sm + wr4 + c_ * 2048 + buf * 18432) =
            deq8(R.q[0][c_], R.q[1][c_], R.q[2][c_], R.q[3][c_], R.q[4][c_], R.q[5][c_], R.q[6][c_], R.q[7][c_], s, c);
      };
      if constexpr (doA) ACC(6, issue_A4(sA, voffA, ldsA + ((i + 2) % 3) * 32768));
      col(IC<0>{}, sa[0], sa[1]);
      if constexpr (doA) ACC(6, issue_A4(sA + 32 * XROW, voffA, ldsA + ((i + 2) % 3) * 32768 + 4096));
      col(IC<1>{}, sa[2], sa[3]);
      if constexpr (doB) ACC(7, issue_B4<0>(L, qb4 + oB, voffQ));
      col(IC<2>{}, sb[0], sb[1]);
      if constexpr (doB) ACC(7, issue_B4<4>(L, qb4 + oB, voffQ));
      col(IC<3>{}, sb[2], sb[3]);
      if constexpr (doB) issue_B1(L, qb1 + oB, voff1);
      u32x2 o1 = deq4(R.t[0][0], R.t[1][0], R.t[0][1], R.t[1][1], s1[0], s1[1], s1[2], s1[3]);
      *(AS3 unsigned*)(sm + wr1a + buf * 18432) = o1[0];
      *(AS3 unsigned*)(sm + wr1b + buf * 18432) = o1[1];
      if constexpr (i >= 1) ACC(4, asm volatile("s_waitcnt vmcnt(%0)" ::"i"(SCH.wA[i + 1]) : "memory"));
      ACC(5, bar());
    };
    interval(IC<-1>{}); interval(IC<0>{}); interval(IC<1>{}); interval(IC<2>{}); interval(IC<3>{});
    interval(IC<4>{}); interval(IC<5>{}); interval(IC<6>{}); interval(IC<7>{});
    MARK(8);
    bar();
  } else {
    STAMP();
    u32x2 sv0, zv0, sv1, zv1;
    {
      int e1 = t + 256 < 324 ? t + 256 : 323;
      unsigned so0 = (unsigned)((t / 36) * N_ + n0 + (t % 36) * 4) * 2;
      unsigned so1 = (unsigned)((e1 / 36) * N_ + n0 + (e1 % 36) * 4) * 2;
      asm volatile("global_load_dwordx2 %0, %1, %2" : "=v"(sv0) : "v"(so0), "s"(sc) : "memory");
      asm volatile("global_load_dwordx2 %0, %1, %2" : "=v"(zv0) : "v"(so0), "s"(zr) : "memory");
      asm volatile("global_load_dwordx2 %0, %1, %2" : "=v"(sv1) : "v"(so1), "s"(sc) : "memory");
      asm volatile("global_load_dwordx2 %0, %1, %2" : "=v"(zv1) : "v"(so1), "s"(zr) : "memory");
    }
    { const char* s = xb + grp(0) * 128; issue_A4(s, voffA, ldsA); issue_A4(s + 32 * XROW, voffA, ldsA + 4096); }
    asm volatile("s_waitcnt vmcnt(8)" : "+v"(sv0), "+v"(zv0), "+v"(sv1), "+v"(zv1)::"memory");
    MARK(1);
    auto tab = [&](u32x2 sv, u32x2 zv, int e) {
      float s0 = bflo(sv[0]), s1 = bfhi(sv[0]), s2 = bflo(sv[1]), s3 = bfhi(sv[1]);
      float z0 = bflo(zv[0]), z1 = bfhi(zv[0]), z2 = bflo(zv[1]), z3 = bfhi(zv[1]);
      f32x4 a = {s0, fma1(-z0, s0, 0.f), s1, fma1(-z1, s1, 0.f)}, c = {s2, fma1(-z2, s2, 0.f), s3, fma1(-z3, s3, 0.f)};
      *(AS3 f32x4*)(sm + LDS_S + e * 32) = a;
      *(AS3 f32x4*)(sm + LDS_S + e * 32 + 16) = c;
    };
    tab(sv0, zv0, t);
    if (t + 256 < 324) tab(sv1, zv1, t + 256);
    bar();
    unsigned short* ob = out + (size_t)(m0 + w * 64 + q * 4) * N_ + n0;
    { const char* s = xb + grp(1) * 128; issue_A4(s, voffA, ldsA + 32768); issue_A4(s + 32 * XROW, voffA, ldsA + 32768 + 4096); }
    if (CPRIO) __builtin_amdgcn_s_setprio(CPRIO);
    f32x4 acc[4][9] = {};
#pragma unroll
    for (int mi = 0; mi < 4; ++mi)
      asm volatile("" : "+v"(acc[mi][0]), "+v"(acc[mi][1]), "+v"(acc[mi][2]), "+v"(acc[mi][3]), "+v"(acc[mi][4]), "+v"(acc[mi][5]), "+v"(acc[mi][6]), "+v"(acc[mi][7]), "+v"(acc[mi][8]));
    asm volatile("" : "+v"(ob));
    const int fr0 = r * 128 + ((q ^ (r & 7)) * 16);
    const int fr1 = r * 128 + (((4 + q) ^ (r & 7)) * 16);
    bf16x8 a0[4], a1[4], bq[6];
    STAMP();
    MARK(2);
    asm volatile("s_waitcnt vmcnt(8)" ::: "memory");
    MARK(3);
    bar();
    MARK(4);
    STAMP();
    int baseA = LDS_A + w * 8192, baseB = LDS_B;
    {
      AS3 char* pa = sm + baseA + fr0; AS3 char* pb = sm + baseB + fr0;
#pragma unroll
      for (int mi = 0; mi < 4; ++mi) a0[mi] = *(AS3 bf16x8*)(pa + mi * 2048);
#pragma unroll
      for (int u = 0; u < 4; ++u) bq[u] = *(AS3 bf16x8*)(pb + u * 2048);
    }
#pragma unroll 1
    for (int i = 0; i < 9; ++i) {
      AS3 char* pa1 = sm + baseA + fr1;
      AS3 char* pb0 = sm + baseB + fr0;
      AS3 char* pb1 = sm + baseB + fr1;
      const int nA = baseA + 32768 >= LDS_A + w * 8192 + 3 * 32768 ? baseA - 65536 : baseA + 32768;
      const int nB = baseB ^ (LDS_B ^ (LDS_B + 18432));
      AS3 char* na0 = sm + nA + fr0;
      AS3 char* nb0 = sm + nB + fr0;
      SB();
#pragma unroll
      for (int u = 0; u < 18; ++u) {
        const int s = u / 9, j = u % 9;
        if (u + 4 < 18) {
          const int u4 = u + 4;
          bq[u4 % 6] = *(AS3 bf16x8*)((u4 < 9 ? pb0 : pb1) + (u4 % 9) * 2048);
        }
        if (u >= 2 && u < 6) a1[u - 2] = *(AS3 bf16x8*)(pa1 + (u - 2) * 2048);
        if (u == 15) {
          STAMP();
          ACC(5, bar_v0());
          STAMP();
#pragma unroll
          for (int mi = 0; mi < 4; ++mi) a0[mi] = *(AS3 bf16x8*)(na0 + mi * 2048);
          bq[0] = *(AS3 bf16x8*)(nb0);
          bq[1] = *(AS3 bf16x8*)(nb0 + 2048);
        }
        if (u == 16) bq[2] = *(AS3 bf16x8*)(nb0 + 2 * 2048);
        if (u == 17) bq[3] = *(AS3 bf16x8*)(nb0 + 3 * 2048);
#pragma unroll
        for (int mi = 0; mi < 4; ++mi)
          acc[mi][j] = __builtin_amdgcn_mfma_f32_16x16x32_bf16(s ? a1[mi] : a0[mi], bq[u % 6], acc[mi][j], 0, 0, 0);
        SB();
      }
      baseA = nA; baseB = nB;
    }
    STAMP();
    MARK(6);
#pragma unroll
    for (int mi = 0; mi < 4; ++mi)
#pragma unroll
      for (int j = 0; j < 4; ++j) {
        unsigned short* orow = ob + (size_t)(mi * 16 + j) * N_;
#pragma unroll
        for (int G = 0; G < 2; ++G) {
          u32x2 o = {pack_bf16(acc[mi][G * 4 + 0][j], acc[mi][G * 4 + 1][j]), pack_bf16(acc[mi][G * 4 + 2][j], acc[mi][G * 4 + 3][j])};
          *(u32x2*)(orow + odd * 16 + G * 64 + r * 4) = o;
        }
        orow[rel1 + r] = (unsigned short)(pack_bf16(acc[mi][8][j], 0.f) & 0xffff);
      }
    MARK(7);
    MARK(8);
  }
}

extern "C" void kernel_launch(void* const* d_in, const int* in_sizes, int n_in, void* d_out, int out_size, void* d_ws,
                              size_t ws_size, hipStream_t stream) {
  const unsigned short* x = (const unsigned short*)d_in[0];
  const int* qw = (const int*)d_in[1];
  const unsigned short* sc = (const unsigned short*)d_in[2];
  const unsigned short* zr = (const unsigned short*)d_in[3];
  wq_gemm_v3<<<256, 512, 0, stream>>>(x, qw, sc, zr, (unsigned short*)d_out
  );
}
```

```cpp
#include <hip/hip_runtime.h>
#include <hip/hip_bf16.h>
#include <stdint.h>

typedef __attribute__((ext_vector_type(4))) unsigned u32x4;
typedef __attribute__((ext_vector_type(4))) int i32x4;
typedef __attribute__((ext_vector_type(2))) int i32x2;
typedef __attribute__((ext_vector_type(2))) unsigned u32x2;
typedef __attribute__((ext_vector_type(4))) float f32x4;
typedef __attribute__((ext_vector_type(8))) short bf16x8;
typedef __attribute__((ext_vector_type(2))) __bf16 bf16x2_t;
typedef __attribute__((ext_vector_type(2))) float f32x2;
#define AS3 __attribute__((address_space(3)))
#define ROT 1
#define MARK(k) do{}while(0)
#define SB() __builtin_amdgcn_sched_barrier(0)

constexpr int M_ = 2048, K_ = 576, N_ = 4608;
constexpr int QROW = N_ * 4;
constexpr int XROW = K_ * 2;
constexpr int OROW = N_ * 2;
constexpr int ASZ = 32768, BSZ = 18432;
constexpr int LDS_A = 0;
constexpr int LDS_B = 3 * ASZ;
constexpr int LDS_S = LDS_B + 2 * BSZ;
constexpr int LDS_TOTAL = LDS_S + 9 * 144 * 8;

__device__ __forceinline__ unsigned pack_bf16(float a, float b) {
  f32x2 v = {a, b};
  bf16x2_t r = __builtin_convertvector(v, bf16x2_t);
  return __builtin_bit_cast(unsigned, r);
}
__device__ __forceinline__ float mul1(float a, float b) {
  float r;
  asm("v_mul_f32 %0, %1, %2" : "=v"(r) : "v"(a), "v"(b));
  return r;
}
__device__ __forceinline__ float bflo(unsigned v) { return __builtin_bit_cast(float, v << 16); }
__device__ __forceinline__ float bfhi(unsigned v) { return __builtin_bit_cast(float, v & 0xffff0000u); }
__device__ __forceinline__ void bar() {
  asm volatile("" ::: "memory");
  __builtin_amdgcn_s_waitcnt(0xC07F);
  __builtin_amdgcn_s_barrier();
  asm volatile("" ::: "memory");
}
__device__ __forceinline__ void deq_rows(i32x4 a, i32x4 b, f32x4 sa, f32x4 sb, unsigned& o0, unsigned& o1, unsigned& o2, unsigned& o3) {
  float t0, t1, t2, t3, t4, t5, t6, t7;
  asm("v_fma_mix_f32 %4, %12, %20, %21 op_sel_hi:[1,0,0]\n\tv_fma_mix_f32 %5, %16, %20, %21 op_sel_hi:[1,0,0]\n\t"
      "v_fma_mix_f32 %6, %13, %22, %23 op_sel_hi:[1,0,0]\n\tv_fma_mix_f32 %7, %17, %22, %23 op_sel_hi:[1,0,0]\n\t"
      "v_fma_mix_f32 %8, %14, %24, %25 op_sel_hi:[1,0,0]\n\tv_fma_mix_f32 %9, %18, %24, %25 op_sel_hi:[1,0,0]\n\t"
      "v_fma_mix_f32 %10, %15, %26, %27 op_sel_hi:[1,0,0]\n\tv_fma_mix_f32 %11, %19, %26, %27 op_sel_hi:[1,0,0]\n\t"
      "v_cvt_pk_bf16_f32 %0, %4, %5\n\tv_cvt_pk_bf16_f32 %1, %6, %7\n\tv_cvt_pk_bf16_f32 %2, %8, %9\n\tv_cvt_pk_bf16_f32 %3, %10, %11"
      : "=&v"(o0), "=&v"(o1), "=&v"(o2), "=&v"(o3), "=&v"(t0), "=&v"(t1), "=&v"(t2), "=&v"(t3), "=&v"(t4), "=&v"(t5), "=&v"(t6), "=&v"(t7)
      : "v"(a[0]), "v"(a[1]), "v"(a[2]), "v"(a[3]), "v"(b[0]), "v"(b[1]), "v"(b[2]), "v"(b[3]),
        "v"(sa[0]), "v"(sa[1]), "v"(sa[2]), "v"(sa[3]), "v"(sb[0]), "v"(sb[1]), "v"(sb[2]), "v"(sb[3]));
}
__device__ __forceinline__ void deq_t1(i32x2 a, i32x2 b, f32x4 s, unsigned& o0, unsigned& o1) {
  float t0, t1, t2, t3;
  asm("v_fma_mix_f32 %2, %6, %10, %11 op_sel_hi:[1,0,0]\n\tv_fma_mix_f32 %3, %8, %10, %11 op_sel_hi:[1,0,0]\n\t"
      "v_fma_mix_f32 %4, %7, %12, %13 op_sel_hi:[1,0,0]\n\tv_fma_mix_f32 %5, %9, %12, %13 op_sel_hi:[1,0,0]\n\t"
      "v_cvt_pk_bf16_f32 %0, %2, %3\n\tv_cvt_pk_bf16_f32 %1, %4, %5"
      : "=&v"(o0), "=&v"(o1), "=&v"(t0), "=&v"(t1), "=&v"(t2), "=&v"(t3)
      : "v"(a[0]), "v"(a[1]), "v"(b[0]), "v"(b[1]), "v"(s[0]), "v"(s[1]), "v"(s[2]), "v"(s[3]));
}
__device__ __forceinline__ void ldq2(i32x4& a, i32x4& b, const char* base, unsigned v0, unsigned v1) {
  asm volatile("global_load_dwordx4 %0, %2, %4\n\tglobal_load_dwordx4 %1, %3, %4" : "=&v"(a), "=&v"(b) : "v"(v0), "v"(v1), "s"(base) : "memory");
}
__device__ __forceinline__ void ldt2(i32x2& a, i32x2& b, const char* base, unsigned v0, unsigned v1) {
  asm volatile("global_load_dwordx2 %0, %2, %4\n\tglobal_load_dwordx2 %1, %3, %4" : "=&v"(a), "=&v"(b) : "v"(v0), "v"(v1), "s"(base) : "memory");
}
__device__ __forceinline__ void dma4(unsigned dst, unsigned v0, unsigned v1, unsigned v2, unsigned v3, const char* sA) {
  asm volatile("s_mov_b32 m0, %5\n\ts_nop 0\n\t"
               "global_load_lds_dwordx4 %0, %4\n\tglobal_load_lds_dwordx4 %1, %4 offset:1024\n\t"
               "global_load_lds_dwordx4 %2, %4 offset:2048\n\tglobal_load_lds_dwordx4 %3, %4 offset:3072"
               :: "v"(v0), "v"(v1), "v"(v2), "v"(v3), "s"(sA), "s"(dst) : "memory");
}

__global__ __launch_bounds__(512, 2) void wq_gemm_v13(const unsigned short* __restrict__ x, const int* __restrict__ qw,
                                                      const unsigned short* __restrict__ sc, const unsigned short* __restrict__ zr,
                                                      unsigned short* __restrict__ out
) {
  asm volatile("" :: "s"(x), "s"(qw), "s"(sc), "s"(zr), "s"(out));
  __shared__ __attribute__((aligned(16))) char smem[LDS_TOTAL];
  AS3 char* sm = (AS3 char*)smem;
  const int b = blockIdx.x;
  const int xcd = b & 7, idx = b >> 3;
  const int nt = xcd * 4 + (idx >> 3), mt = idx & 7;
  const int t = threadIdx.x, l = t & 63;
  const int w = __builtin_amdgcn_readfirstlane(t >> 6);
  const int r = l & 15, q = l >> 4;
  const int n0 = nt * 144, m0 = mt * 256;
  const int odd = nt & 1;
  const int rel1 = odd ? 0 : 128;
  const int p = w & 3;

  if (w >= 4) {
    int g = ROT ? (mt + nt) % 9 : 0;
    const int G = p >> 1, h = p & 1;
    const int rel4 = odd * 16 + G * 64;
    const int cq = r, kgl = q;
    const int cp = l & 7, kp = l >> 3;
    const char* qb = (const char*)qw + (size_t)n0 * 4;
    const char* xb = (const char*)x + (size_t)(m0 + p * 64) * XROW;
    unsigned vq[8], vt[2], va[4];
#pragma unroll
    for (int j = 0; j < 8; ++j) vq[j] = (unsigned)((h * 32 + kgl * 8 + j) * QROW + rel4 * 4 + cq * 16);
    vt[0] = (unsigned)((p * 16 + kp * 2) * QROW + rel1 * 4 + cp * 8); vt[1] = vt[0] + QROW;
#pragma unroll
    for (int j = 0; j < 4; ++j) va[j] = (unsigned)((l >> 3) * XROW + (((l & 7) ^ (l >> 3)) * 16) + j * (8 * XROW - 1024));
    const unsigned ldsA = (unsigned)(size_t)sm + LDS_A + p * 8192;
    const int kg = h * 4 + kgl;
    const int kc1 = p * 2 + (kp >> 2);
    int wr4 = LDS_B + G * 4 * 2048 + cq * 128 + ((kg ^ (cq & 7)) * 16);
    int wr1a = LDS_B + 8 * 2048 + (cp * 2) * 128 + ((kc1 ^ ((cp * 2) & 7)) * 16) + (kp & 3) * 4;
    int wr1b = LDS_B + 8 * 2048 + (cp * 2 + 1) * 128 + ((kc1 ^ ((cp * 2 + 1) & 7)) * 16) + (kp & 3) * 4;
    const int sc4 = LDS_S + (rel4 + cq * 4) * 8;
    const int sc1 = LDS_S + (rel1 + cp * 2) * 8;
    i32x4 Q[8] = {}; i32x2 T[2] = {};
    f32x4 sa = {}, sb = {}, s1 = {};
    unsigned m0A = ldsA;
    int dB = BSZ;
#pragma unroll 1
    for (int s = -1; s < 9; ++s) {
      const bool ld = s < 8;
      const char* sA = xb + g * 128;
      const char* sB = qb + (size_t)g * (64 * QROW);
      asm volatile("s_waitcnt vmcnt(0)" : "+v"(Q[0]), "+v"(Q[1]), "+v"(Q[2]), "+v"(Q[3]), "+v"(Q[4]), "+v"(Q[5]), "+v"(Q[6]), "+v"(Q[7]), "+v"(T[0]), "+v"(T[1]) :: "memory");
      unsigned o[4][4], u0, u1;
      deq_rows(Q[0], Q[1], sa, sb, o[0][0], o[1][0], o[2][0], o[3][0]);
      if (ld) ldq2(Q[0], Q[1], sB, vq[0], vq[1]);
      deq_rows(Q[2], Q[3], sa, sb, o[0][1], o[1][1], o[2][1], o[3][1]);
      if (ld) ldq2(Q[2], Q[3], sB, vq[2], vq[3]);
      deq_rows(Q[4], Q[5], sa, sb, o[0][2], o[1][2], o[2][2], o[3][2]);
      if (ld) ldq2(Q[4], Q[5], sB, vq[4], vq[5]);
      deq_rows(Q[6], Q[7], sa, sb, o[0][3], o[1][3], o[2][3], o[3][3]);
      deq_t1(T[0], T[1], s1, u0, u1);
#pragma unroll
      for (int c = 0; c < 4; ++c) { u32x4 v = {o[c][0], o[c][1], o[c][2], o[c][3]}; *(AS3 u32x4*)(sm + wr4 + dB + c * 2048) = v; }
      *(AS3 unsigned*)(sm + wr1a + dB) = u0;
      *(AS3 unsigned*)(sm + wr1b + dB) = u1;
      if (ld) {
        ldq2(Q[6], Q[7], sB, vq[6], vq[7]); ldt2(T[0], T[1], sB, vt[0], vt[1]);
        dma4(m0A, va[0], va[1], va[2], va[3], sA);
        dma4(m0A + 4096, va[0], va[1], va[2], va[3], sA + 32 * XROW);
      }
      bar();
      sa = *(AS3 f32x4*)(sm + sc4 + g * 1152);
      sb = *(AS3 f32x4*)(sm + sc4 + g * 1152 + 16);
      s1 = *(AS3 f32x4*)(sm + sc1 + g * 1152);
      g = g == 8 ? 0 : g + 1;
      m0A = m0A == ldsA + 2 * ASZ ? ldsA : m0A + ASZ;
      dB ^= BSZ;
    }
    bar();
    MARK(8);
  } else {
#pragma unroll 1
    for (int e = t; e < 324; e += 256) {
      const size_t so = (size_t)(e / 36) * N_ + n0 + (e % 36) * 4;
      const u32x2 sv = *(const u32x2*)(sc + so), zv = *(const u32x2*)(zr + so);
      const float s0 = bflo(sv[0]), s1 = bfhi(sv[0]), s2 = bflo(sv[1]), s3 = bfhi(sv[1]);
      const float k = 16777216.f;
      f32x4 a = {mul1(s0, k), mul1(-bflo(zv[0]), s0), mul1(s1, k), mul1(-bfhi(zv[0]), s1)};
      f32x4 c = {mul1(s2, k), mul1(-bflo(zv[1]), s2), mul1(s3, k), mul1(-bfhi(zv[1]), s3)};
      *(AS3 f32x4*)(sm + LDS_S + e * 32) = a;
      *(AS3 f32x4*)(sm + LDS_S + e * 32 + 16) = c;
    }
    MARK(1);
    bar();
    f32x4 acc[4][9] = {};
#pragma unroll
    for (int mi = 0; mi < 4; ++mi)
      asm volatile("" : "+v"(acc[mi][0]), "+v"(acc[mi][1]), "+v"(acc[mi][2]), "+v"(acc[mi][3]), "+v"(acc[mi][4]), "+v"(acc[mi][5]), "+v"(acc[mi][6]), "+v"(acc[mi][7]), "+v"(acc[mi][8]) :: "memory");
    char* ob = (char*)(out + (size_t)(m0 + w * 64) * N_ + n0);
    const unsigned vo4 = (unsigned)(q * 4 * OROW + (odd * 16 + r * 4) * 2);
    const unsigned vo1 = (unsigned)(q * 4 * OROW + (rel1 + r) * 2);
    const int fr0 = r * 128 + ((q ^ (r & 7)) * 16);
    const int fr1 = r * 128 + (((4 + q) ^ (r & 7)) * 16);
    bf16x8 a0[4], a1[4], bq[6];
    int baseA = LDS_A + w * 8192, baseB = LDS_B;
    MARK(2);
    bar();
    MARK(4);
#define TS(u) ((u) < 8 ? (u) % 4 : (((u) - 8) % 5 == 0 ? 8 : 3 + ((u) - 8) % 5))
#define KS(u) ((u) < 8 ? (u) / 4 : ((u) - 8) / 5)
    {
      AS3 char* pa = sm + baseA + fr0; AS3 char* pb = sm + baseB + fr0;
#pragma unroll
      for (int mi = 0; mi < 4; ++mi) a0[mi] = *(AS3 bf16x8*)(pa + mi * 2048);
#pragma unroll
      for (int u = 0; u < 4; ++u) bq[u] = *(AS3 bf16x8*)(pb + u * 2048);
    }
#define ST4(G, mi, j) do { \
      u32x2 o_ = {pack_bf16(acc[mi][G * 4 + 0][j], acc[mi][G * 4 + 1][j]), pack_bf16(acc[mi][G * 4 + 2][j], acc[mi][G * 4 + 3][j])}; \
      asm volatile("global_store_dwordx2 %0, %1, %2 offset:%3" :: "v"(vo4), "v"(o_), "s"(ob + ((mi) * 16 + (j)) * OROW), "i"(G * 128) : "memory"); } while (0)
#pragma unroll 1
    for (int i = 0; i < 9; ++i) {
      AS3 char* pa1 = sm + baseA + fr1;
      AS3 char* pb0 = sm + baseB + fr0;
      AS3 char* pb1 = sm + baseB + fr1;
      const int nA = baseA + ASZ >= LDS_A + w * 8192 + 3 * ASZ ? baseA - 2 * ASZ : baseA + ASZ;
      const int nB = baseB ^ (LDS_B ^ (LDS_B + BSZ));
      AS3 char* na0 = sm + nA + fr0;
      AS3 char* nb0 = sm + nB + fr0;
      SB();
#pragma unroll
      for (int u = 0; u < 18; ++u) {
        const int s = KS(u), j = TS(u);
#define MF(mi) acc[mi][j] = __builtin_amdgcn_mfma_f32_16x16x32_bf16(s ? a1[mi] : a0[mi], bq[u % 6], acc[mi][j], 0, 0, 0); SB()
        if (u == 15) { bar(); SB(); }
        MF(0);
        if (u + 4 < 18) {
          const int u4 = u + 4;
          bq[u4 % 6] = *(AS3 bf16x8*)((KS(u4) ? pb1 : pb0) + TS(u4) * 2048);
        }
        if (u == 15) a0[0] = *(AS3 bf16x8*)(na0);
        if (u == 16) a0[3] = *(AS3 bf16x8*)(na0 + 3 * 2048);
        if (u == 17) bq[3] = *(AS3 bf16x8*)(nb0 + 3 * 2048);
        SB();
        MF(1);
        if (u < 2) a1[2 * u] = *(AS3 bf16x8*)(pa1 + (2 * u) * 2048);
        if (u == 15) bq[0] = *(AS3 bf16x8*)(nb0);
        if (u == 16) bq[1] = *(AS3 bf16x8*)(nb0 + 2048);
        SB();
        MF(2);
        if (u < 2) a1[2 * u + 1] = *(AS3 bf16x8*)(pa1 + (2 * u + 1) * 2048);
        if (u == 15) a0[1] = *(AS3 bf16x8*)(na0 + 2048);
        if (u == 16) bq[2] = *(AS3 bf16x8*)(nb0 + 2 * 2048);
        SB();
        MF(3);
        if (u == 15) a0[2] = *(AS3 bf16x8*)(na0 + 2 * 2048);
        if (u >= 9 && u < 17 && (u & 1)) {
          if (i == 8) { const int mi = (u - 9) / 2; ST4(0, mi, 0); ST4(0, mi, 1); ST4(0, mi, 2); ST4(0, mi, 3); }
        }
        SB();
      }
      baseA = nA; baseB = nB;
    }
    MARK(6);
#pragma unroll
    for (int mi = 0; mi < 4; ++mi)
#pragma unroll
      for (int j = 0; j < 4; ++j) {
        ST4(1, mi, j);
        asm volatile("global_store_short %0, %1, %2" :: "v"(vo1), "v"(pack_bf16(acc[mi][8][j], acc[mi][8][j])), "s"(ob + (mi * 16 + j) * OROW) : "memory");
      }
    MARK(7);
    MARK(8);
  }
}

extern "C" void kernel_launch(void* const* d_in, const int* in_sizes, int n_in, void* d_out, int out_size, void* d_ws,
                              size_t ws_size, hipStream_t stream) {
  const unsigned short* x = (const unsigned short*)d_in[0];
  const int* qw = (const int*)d_in[1];
  const unsigned short* sc = (const unsigned short*)d_in[2];
  const unsigned short* zr = (const unsigned short*)d_in[3];
  wq_gemm_v13<<<256, 512, 0, stream>>>(x, qw, sc, zr, (unsigned short*)d_out
  );
}
```

```cpp
#include <hip/hip_runtime.h>
#include <hip/hip_bf16.h>
#include <stdint.h>
#include <type_traits>

typedef __attribute__((ext_vector_type(4))) unsigned u32x4;
typedef __attribute__((ext_vector_type(4))) int i32x4;
typedef __attribute__((ext_vector_type(2))) int i32x2;
typedef __attribute__((ext_vector_type(2))) unsigned u32x2;
typedef __attribute__((ext_vector_type(4))) float f32x4;
typedef __attribute__((ext_vector_type(8))) short bf16x8;
typedef __attribute__((ext_vector_type(2))) __bf16 bf16x2_t;
typedef __attribute__((ext_vector_type(2))) float f32x2;
#define AS3 __attribute__((address_space(3)))
#define SB() __builtin_amdgcn_sched_barrier(0)

constexpr int M_ = 2048, K_ = 576, N_ = 4608;
constexpr int QROW = N_ * 4;
constexpr int XROW = K_ * 2;
constexpr int LDS_A = 0;
constexpr int LDS_B = 3 * 32768;
constexpr int LDS_S = LDS_B + 2 * 18432;
constexpr int LDS_TOTAL = LDS_S + 9 * 144 * 8 + 256;

__device__ __forceinline__ unsigned pack_bf16(float a, float b) {
  f32x2 v = {a, b};
  bf16x2_t r = __builtin_convertvector(v, bf16x2_t);
  return __builtin_bit_cast(unsigned, r);
}
__device__ __forceinline__ float mul1(float a, float b) {
  float r;
  asm("v_mul_f32 %0, %1, %2" : "=v"(r) : "v"(a), "v"(b));
  return r;
}
__device__ __forceinline__ float bflo(unsigned v) { return __builtin_bit_cast(float, v << 16); }
__device__ __forceinline__ float bfhi(unsigned v) { return __builtin_bit_cast(float, v & 0xffff0000u); }
__device__ __forceinline__ void bar() {
  asm volatile("" ::: "memory");
  __builtin_amdgcn_s_waitcnt(0xC07F);
  __builtin_amdgcn_s_barrier();
  asm volatile("" ::: "memory");
}
__device__ __forceinline__ void deq_rows(i32x4 a, i32x4 b, f32x4 sa, f32x4 sb, unsigned& o0, unsigned& o1, unsigned& o2, unsigned& o3) {
  float t0, t1, t2, t3, t4, t5, t6, t7;
  asm("v_fma_mix_f32 %4, %12, %20, %21 op_sel_hi:[1,0,0]\n\tv_fma_mix_f32 %5, %16, %20, %21 op_sel_hi:[1,0,0]\n\t"
      "v_fma_mix_f32 %6, %13, %22, %23 op_sel_hi:[1,0,0]\n\tv_fma_mix_f32 %7, %17, %22, %23 op_sel_hi:[1,0,0]\n\t"
      "v_fma_mix_f32 %8, %14, %24, %25 op_sel_hi:[1,0,0]\n\tv_fma_mix_f32 %9, %18, %24, %25 op_sel_hi:[1,0,0]\n\t"
      "v_fma_mix_f32 %10, %15, %26, %27 op_sel_hi:[1,0,0]\n\tv_fma_mix_f32 %11, %19, %26, %27 op_sel_hi:[1,0,0]\n\t"
      "v_cvt_pk_bf16_f32 %0, %4, %5\n\tv_cvt_pk_bf16_f32 %1, %6, %7\n\tv_cvt_pk_bf16_f32 %2, %8, %9\n\tv_cvt_pk_bf16_f32 %3, %10, %11"
      : "=&v"(o0), "=&v"(o1), "=&v"(o2), "=&v"(o3), "=&v"(t0), "=&v"(t1), "=&v"(t2), "=&v"(t3), "=&v"(t4), "=&v"(t5), "=&v"(t6), "=&v"(t7)
      : "v"(a[0]), "v"(a[1]), "v"(a[2]), "v"(a[3]), "v"(b[0]), "v"(b[1]), "v"(b[2]), "v"(b[3]),
        "v"(sa[0]), "v"(sa[1]), "v"(sa[2]), "v"(sa[3]), "v"(sb[0]), "v"(sb[1]), "v"(sb[2]), "v"(sb[3]));
}
__device__ __forceinline__ void deq_t1(i32x2 a, i32x2 b, f32x4 s, unsigned& o0, unsigned& o1) {
  float t0, t1, t2, t3;
  asm("v_fma_mix_f32 %2, %6, %10, %11 op_sel_hi:[1,0,0]\n\tv_fma_mix_f32 %3, %8, %10, %11 op_sel_hi:[1,0,0]\n\t"
      "v_fma_mix_f32 %4, %7, %12, %13 op_sel_hi:[1,0,0]\n\tv_fma_mix_f32 %5, %9, %12, %13 op_sel_hi:[1,0,0]\n\t"
      "v_cvt_pk_bf16_f32 %0, %2, %3\n\tv_cvt_pk_bf16_f32 %1, %4, %5"
      : "=&v"(o0), "=&v"(o1), "=&v"(t0), "=&v"(t1), "=&v"(t2), "=&v"(t3)
      : "v"(a[0]), "v"(a[1]), "v"(b[0]), "v"(b[1]), "v"(s[0]), "v"(s[1]), "v"(s[2]), "v"(s[3]));
}

struct BRegs { i32x4 q[8]; i32x2 t[2]; };
__device__ __forceinline__ void ldq2(i32x4& a, i32x4& b, const char* base, unsigned v0, unsigned v1) {
  asm volatile("global_load_dwordx4 %0, %2, %4\n\tglobal_load_dwordx4 %1, %3, %4" : "=&v"(a), "=&v"(b) : "v"(v0), "v"(v1), "s"(base) : "memory");
}
__device__ __forceinline__ void ldt2(i32x2& a, i32x2& b, const char* base, unsigned v0, unsigned v1) {
  asm volatile("global_load_dwordx2 %0, %2, %4\n\tglobal_load_dwordx2 %1, %3, %4" : "=&v"(a), "=&v"(b) : "v"(v0), "v"(v1), "s"(base) : "memory");
}
__device__ __forceinline__ void pin_B(BRegs& r) {
  asm volatile("" : "+v"(r.q[0]), "+v"(r.q[1]), "+v"(r.q[2]), "+v"(r.q[3]), "+v"(r.q[4]), "+v"(r.q[5]), "+v"(r.q[6]), "+v"(r.q[7]),
                 "+v"(r.t[0]), "+v"(r.t[1]) :: "memory");
}
__device__ __forceinline__ void set_m0(unsigned v) { asm volatile("s_mov_b32 m0, %0\n\ts_nop 0" :: "s"(v) : "memory"); }
template <int J>
__device__ __forceinline__ void dma2(const char* xg, unsigned v) {
  const char* p0 = xg + J * (8 * XROW - 1024); const char* p1 = xg + (J + 1) * (8 * XROW - 1024);
  asm volatile("global_load_lds_dwordx4 %0, %1 offset:%3\n\tglobal_load_lds_dwordx4 %0, %2 offset:%4"
      :: "v"(v), "s"(p0), "s"(p1), "i"(J * 1024), "i"((J + 1) * 1024) : "memory");
}
__device__ __forceinline__ void dma8(const char* xs, unsigned v, unsigned ldsdst) {
  set_m0(ldsdst); dma2<0>(xs, v); dma2<2>(xs, v);
  set_m0(ldsdst + 4096); dma2<0>(xs + 32 * XROW, v); dma2<2>(xs + 32 * XROW, v);
}

template <int J>
__device__ __forceinline__ void dmaPc(unsigned v0, unsigned v1, const char* sA) {
  asm volatile("global_load_lds_dwordx4 %0, %2 offset:%3\n\tglobal_load_lds_dwordx4 %1, %2 offset:%4"
               :: "v"(v0), "v"(v1), "s"(sA), "i"((J & 3) * 1024), "i"(((J + 1) & 3) * 1024) : "memory");
}
template <int I> using IC = std::integral_constant<int, I>;

__global__ __launch_bounds__(512, 2) void wq_gemm_kernel(const unsigned short* __restrict__ x, const int* __restrict__ qw,
                                                     const unsigned short* __restrict__ sc, const unsigned short* __restrict__ zr,
                                                     unsigned short* __restrict__ out) {
  asm volatile("" :: "s"(x), "s"(qw), "s"(sc), "s"(zr), "s"(out));
  __shared__ __attribute__((aligned(16))) char smem[LDS_TOTAL];
  AS3 char* sm = (AS3 char*)smem;
  const unsigned lds0 = (unsigned)(size_t)sm;
  const int b = blockIdx.x;
  const int xcd = b & 7, idx = b >> 3;
  const int nt = xcd * 4 + (idx >> 3), mt = idx & 7;
  const int t = threadIdx.x, l = t & 63;
  const int w = __builtin_amdgcn_readfirstlane(t >> 6);
  const int r = l & 15, q = l >> 4;
  const int n0 = nt * 144, m0 = mt * 256;
  const int odd = nt & 1;
  const int rel1 = odd ? 0 : 128;
  const int g0 = (mt + nt) % 9;
  auto nxt = [](int g) { return g == 8 ? 0 : g + 1; };
  const int p = w & 3;
  const unsigned voffA0 = (unsigned)((l >> 3) * XROW + (((l & 7) ^ (l >> 3)) * 16));
  const char* xb = (const char*)x + (size_t)(m0 + p * 64) * XROW;
  const unsigned ldsA = lds0 + LDS_A + p * 8192;

  if (w < 4) {
    u32x2 sv0, zv0, sv1, zv1;
    {
      int e1 = t + 256 < 324 ? t + 256 : 323;
      unsigned so0 = (unsigned)((t / 36) * N_ + n0 + (t % 36) * 4) * 2;
      unsigned so1 = (unsigned)((e1 / 36) * N_ + n0 + (e1 % 36) * 4) * 2;
      asm volatile("global_load_dwordx2 %0, %1, %2" : "=v"(sv0) : "v"(so0), "s"(sc) : "memory");
      asm volatile("global_load_dwordx2 %0, %1, %2" : "=v"(zv0) : "v"(so0), "s"(zr) : "memory");
      asm volatile("global_load_dwordx2 %0, %1, %2" : "=v"(sv1) : "v"(so1), "s"(sc) : "memory");
      asm volatile("global_load_dwordx2 %0, %1, %2" : "=v"(zv1) : "v"(so1), "s"(zr) : "memory");
    }
    f32x4 acc[4][9] = {};
#pragma unroll
    for (int mi = 0; mi < 4; ++mi)
      asm volatile("" : "+v"(acc[mi][0]), "+v"(acc[mi][1]), "+v"(acc[mi][2]), "+v"(acc[mi][3]), "+v"(acc[mi][4]), "+v"(acc[mi][5]), "+v"(acc[mi][6]), "+v"(acc[mi][7]), "+v"(acc[mi][8]) :: "memory");
    dma8(xb + g0 * 128, voffA0, ldsA);
    asm volatile("s_waitcnt vmcnt(8)" : "+v"(sv0), "+v"(zv0), "+v"(sv1), "+v"(zv1)::"memory");
    auto tab = [&](u32x2 sv, u32x2 zv, int e) {
      float s0 = bflo(sv[0]), s1 = bfhi(sv[0]), s2 = bflo(sv[1]), s3 = bfhi(sv[1]);
      float z0 = bflo(zv[0]), z1 = bfhi(zv[0]), z2 = bflo(zv[1]), z3 = bfhi(zv[1]);
      const float k = 16777216.f;
      f32x4 a = {mul1(s0, k), mul1(-z0, s0), mul1(s1, k), mul1(-z1, s1)}, c = {mul1(s2, k), mul1(-z2, s2), mul1(s3, k), mul1(-z3, s3)};
      *(AS3 f32x4*)(sm + LDS_S + e * 32) = a;
      *(AS3 f32x4*)(sm + LDS_S + e * 32 + 16) = c;
    };
    tab(sv0, zv0, t);
    if (t + 256 < 324) tab(sv1, zv1, t + 256);
    bar();
    dma8(xb + nxt(g0) * 128, voffA0, ldsA + 32768);
    unsigned short* ob = out + (size_t)(m0 + w * 64 + q * 4) * N_ + n0;
    asm volatile("" : "+v"(ob));
    const int fr0 = r * 128 + ((q ^ (r & 7)) * 16);
    const int fr1 = r * 128 + (((4 + q) ^ (r & 7)) * 16);
    bf16x8 a0[4], a1[4], bq[6];
    asm volatile("s_waitcnt vmcnt(8)" ::: "memory");
    bar();
    int baseA = LDS_A + w * 8192, baseB = LDS_B;
#define TS(u) ((u) < 8 ? (u) % 4 : ((u) < 10 ? 8 : 4 + ((u) - 10) % 4))
#define KS(u) ((u) < 8 ? (u) / 4 : ((u) < 10 ? (u) - 8 : ((u) - 10) / 4))
    {
      AS3 char* pa = sm + baseA + fr0; AS3 char* pb = sm + baseB + fr0;
#pragma unroll
      for (int mi = 0; mi < 4; ++mi) a0[mi] = *(AS3 bf16x8*)(pa + mi * 2048);
#pragma unroll
      for (int u = 0; u < 4; ++u) bq[u] = *(AS3 bf16x8*)(pb + u * 2048);
    }
    auto store_g = [&](auto Gc, int mi, int j) {
      constexpr int G = decltype(Gc)::value;
      u32x2 o = {pack_bf16(acc[mi][G * 4 + 0][j], acc[mi][G * 4 + 1][j]), pack_bf16(acc[mi][G * 4 + 2][j], acc[mi][G * 4 + 3][j])};
      unsigned short* pp = ob + (size_t)(mi * 16 + j) * N_ + odd * 16 + G * 64 + r * 4;
      asm volatile("global_store_dwordx2 %0, %1, off nt" :: "v"(pp), "v"(o) : "memory");
    };
    auto slot = [&](auto lastc) {
      constexpr bool last = decltype(lastc)::value;
      AS3 char* pa1 = sm + baseA + fr1;
      AS3 char* pb0 = sm + baseB + fr0;
      AS3 char* pb1 = sm + baseB + fr1;
      const int nA = baseA + 32768 >= LDS_A + w * 8192 + 3 * 32768 ? baseA - 65536 : baseA + 32768;
      const int nB = baseB ^ (LDS_B ^ (LDS_B + 18432));
      AS3 char* na0 = sm + nA + fr0;
      AS3 char* nb0 = sm + nB + fr0;
      bf16x8 f8[8];
      SB();
#pragma unroll
      for (int u = 0; u < (last ? 10 : 18); ++u) {
        const int s = KS(u), j = TS(u);
#define MF(mi) acc[mi][j] = (j == 8) ? __builtin_amdgcn_mfma_f32_16x16x32_bf16(bq[u % 6], s ? a1[mi] : a0[mi], acc[mi][j], 0, 0, 0) \
                                     : __builtin_amdgcn_mfma_f32_16x16x32_bf16(s ? a1[mi] : a0[mi], bq[u % 6], acc[mi][j], 0, 0, 0); SB()
        if (u == 15 && !last) { asm volatile("s_waitcnt vmcnt(0)" ::: "memory"); bar(); SB(); }
        MF(0);
        if (u + 4 < (last ? 10 : 18)) {
          const int u4 = u + 4;
          bq[u4 % 6] = *(AS3 bf16x8*)((KS(u4) ? pb1 : pb0) + TS(u4) * 2048);
        }
        if (!last) {
          if (u == 15) a0[0] = *(AS3 bf16x8*)(na0);
          if (u == 16) a0[3] = *(AS3 bf16x8*)(na0 + 3 * 2048);
          if (u == 17) bq[3] = *(AS3 bf16x8*)(nb0 + 3 * 2048);
        }

        SB();
        MF(1);
        if (u < 2) a1[2 * u] = *(AS3 bf16x8*)(pa1 + (2 * u) * 2048);
        if (!last) {
          if (u == 15) bq[0] = *(AS3 bf16x8*)(nb0);
          if (u == 16) bq[1] = *(AS3 bf16x8*)(nb0 + 2048);
        }
        SB();
        MF(2);
        if (u < 2) a1[2 * u + 1] = *(AS3 bf16x8*)(pa1 + (2 * u + 1) * 2048);
        if (!last) {
          if (u == 15) a0[1] = *(AS3 bf16x8*)(na0 + 2048);
          if (u == 16) bq[2] = *(AS3 bf16x8*)(nb0 + 2 * 2048);
        }
        if (last && u >= 2) f8[u - 2] = *(AS3 bf16x8*)(((u - 2) >> 2 ? pb1 : pb0) + (4 + ((u - 2) & 3)) * 2048);
        SB();
        MF(3);
        if (u == 15 && !last) a0[2] = *(AS3 bf16x8*)(na0 + 2 * 2048);
        SB();
      }
      if (last) {
#pragma unroll
        for (int mi = 0; mi < 4; ++mi) {
#pragma unroll
          for (int e = 0; e < 8; ++e) {
            acc[mi][4 + (e & 3)] = __builtin_amdgcn_mfma_f32_16x16x32_bf16(e >> 2 ? a1[mi] : a0[mi], f8[e], acc[mi][4 + (e & 3)], 0, 0, 0);
            SB();
            if (mi < 2) store_g(IC<0>{}, mi * 2 + (e >> 2), e & 3);
            if (mi == 2 && (e & 1) == 0) {
              const int m1 = e >> 1;
              u32x2 o = {pack_bf16(acc[m1][8][0], acc[m1][8][1]), pack_bf16(acc[m1][8][2], acc[m1][8][3])};
              unsigned short* pp = out + (size_t)(m0 + w * 64 + m1 * 16 + r) * N_ + n0 + rel1 + q * 4;
              asm volatile("global_store_dwordx2 %0, %1, off" :: "v"(pp), "v"(o) : "memory");
            }
            if (mi == 2 && (e & 1)) store_g(IC<1>{}, 0, e >> 1);
            if (mi == 3) store_g(IC<1>{}, 1 + (e >> 2), e & 3);
            SB();
          }
        }
      }
      baseA = nA; baseB = nB;
    };
#pragma unroll 1
    for (int i = 0; i < 8; ++i) slot(std::false_type{});
    slot(std::true_type{});
#pragma unroll
    for (int j = 0; j < 4; ++j) store_g(IC<1>{}, 3, j);
  } else {
    const int G = p >> 1, h = p & 1;
    const int rel4 = odd * 16 + G * 64;
    const int cq = r, kgl = q;
    const int cp = l & 7, kp = l >> 3;
    const char* qb = (const char*)qw + (size_t)n0 * 4;
    unsigned vq[8], vt[2], va[8];
#pragma unroll
    for (int j = 0; j < 8; ++j) vq[j] = (unsigned)((h * 32 + kgl * 8 + j) * QROW + rel4 * 4 + cq * 16);
    vt[0] = (unsigned)((p * 16 + kp * 2) * QROW + rel1 * 4 + cp * 8); vt[1] = vt[0] + QROW;
    BRegs br0, br1;
    auto issB_all = [&](BRegs& R, const char* base) {
#pragma unroll
      for (int jp = 0; jp < 4; ++jp) ldq2(R.q[2 * jp], R.q[2 * jp + 1], base, vq[2 * jp], vq[2 * jp + 1]);
      ldt2(R.t[0], R.t[1], base, vt[0], vt[1]);
    };
    issB_all(br0, qb + (size_t)g0 * (64 * QROW));
    issB_all(br1, qb + (size_t)nxt(g0) * (64 * QROW));
#pragma unroll
    for (int j = 0; j < 8; ++j) va[j] = voffA0 + j * 8 * XROW - (j & 3) * 1024;
    const int kg = h * 4 + kgl;
    const int wr4 = LDS_B + G * 4 * 2048 + cq * 128 + ((kg ^ (cq & 7)) * 16);
    const int kc1 = p * 2 + (kp >> 2);
    const int wr1a = LDS_B + 8 * 2048 + (cp * 2) * 128 + ((kc1 ^ ((cp * 2) & 7)) * 16) + (kp & 3) * 4;
    const int wr1b = LDS_B + 8 * 2048 + (cp * 2 + 1) * 128 + ((kc1 ^ ((cp * 2 + 1) & 7)) * 16) + (kp & 3) * 4;
    const int sc4 = LDS_S + (rel4 + cq * 4) * 8;
    const int sc1 = LDS_S + (rel1 + cp * 2) * 8;
    bar();
    f32x4 sa = *(AS3 f32x4*)(sm + sc4 + g0 * 1152);
    f32x4 sb = *(AS3 f32x4*)(sm + sc4 + g0 * 1152 + 16);
    f32x4 s1 = *(AS3 f32x4*)(sm + sc1 + g0 * 1152);
    asm volatile("s_waitcnt vmcnt(10)" ::: "memory");
    int gA = nxt(g0);
    unsigned m0A = ldsA + 32768;
    auto interval = [&](auto doAc, auto doBc, BRegs& R, int bufoff) {
      constexpr bool doA = decltype(doAc)::value, doB = decltype(doBc)::value;
      pin_B(R);
      const int gB = nxt(gA);
      const char* sA = xb + gA * 128;
      const char* sB = qb + (size_t)gB * (64 * QROW);
      unsigned o[4][4];
      if (doA) { set_m0(m0A); dmaPc<0>(va[0], va[1], sA); }
      deq_rows(R.q[0], R.q[1], sa, sb, o[0][0], o[1][0], o[2][0], o[3][0]);
      if (doB) ldq2(R.q[0], R.q[1], sB, vq[0], vq[1]);
      if (doA) dmaPc<2>(va[2], va[3], sA);
      deq_rows(R.q[2], R.q[3], sa, sb, o[0][1], o[1][1], o[2][1], o[3][1]);
      if (doB) ldq2(R.q[2], R.q[3], sB, vq[2], vq[3]);
      if (doA) { set_m0(m0A + 4096); dmaPc<4>(va[4], va[5], sA); }
      deq_rows(R.q[4], R.q[5], sa, sb, o[0][2], o[1][2], o[2][2], o[3][2]);
      if (doB) ldq2(R.q[4], R.q[5], sB, vq[4], vq[5]);
      deq_rows(R.q[6], R.q[7], sa, sb, o[0][3], o[1][3], o[2][3], o[3][3]);
#pragma unroll
      for (int c = 0; c < 4; ++c) { u32x4 v = {o[c][0], o[c][1], o[c][2], o[c][3]};
        *(AS3 u32x4*)(sm + wr4 + c * 2048 + bufoff) = v;
      }
      unsigned u0, u1;
      deq_t1(R.t[0], R.t[1], s1, u0, u1);
      *(AS3 unsigned*)(sm + wr1a + bufoff) = u0;
      *(AS3 unsigned*)(sm + wr1b + bufoff) = u1;
      sa = *(AS3 f32x4*)(sm + sc4 + gA * 1152);
      sb = *(AS3 f32x4*)(sm + sc4 + gA * 1152 + 16);
      s1 = *(AS3 f32x4*)(sm + sc1 + gA * 1152);
      if (doA) dmaPc<6>(va[6], va[7], sA);
      if (doB) { ldq2(R.q[6], R.q[7], sB, vq[6], vq[7]); ldt2(R.t[0], R.t[1], sB, vt[0], vt[1]); }
      gA = gB; m0A = m0A == ldsA + 65536 ? ldsA : m0A + 32768;
    };
    using T_ = std::true_type; using F_ = std::false_type;
    {
      interval(F_{}, F_{}, br0, 0);
      bar();
      issB_all(br0, qb + (size_t)gA * (64 * QROW));
      asm volatile("s_waitcnt vmcnt(10)" ::: "memory");
    }
#pragma unroll 1
    for (int it = 0; it < 3; ++it) {
      interval(T_{}, T_{}, br1, 18432);
      asm volatile("s_waitcnt vmcnt(18)" ::: "memory");
      bar();
      interval(T_{}, T_{}, br0, 0);
      asm volatile("s_waitcnt vmcnt(18)" ::: "memory");
      bar();
    }
    interval(T_{}, F_{}, br1, 18432);
    asm volatile("s_waitcnt vmcnt(8)" ::: "memory");
    bar();
    interval(F_{}, F_{}, br0, 0);
    asm volatile("s_waitcnt vmcnt(0)" ::: "memory");
    bar();
  }
}

extern "C" void kernel_launch(void* const* d_in, const int* in_sizes, int n_in, void* d_out, int out_size, void* d_ws,
                              size_t ws_size, hipStream_t stream) {
  const unsigned short* x = (const unsigned short*)d_in[0];
  const int* qw = (const int*)d_in[1];
  const unsigned short* sc = (const unsigned short*)d_in[2];
  const unsigned short* zr = (const unsigned short*)d_in[3];
  wq_gemm_kernel<<<256, 512, 0, stream>>>(x, qw, sc, zr, (unsigned short*)d_out);
}
```
